```python
import torch

def _softmax(x, axis=-1):
    return torch.softmax(x, dim=axis)
def _gelu(x):
    return torch.nn.functional.gelu(x, approximate="tanh")
def _silu(x):
    return torch.nn.functional.silu(x)
def _sigmoid(x):
    return torch.sigmoid(x)
def _layernorm(x, axis=-1, eps=1e-5):
    m = x.mean(dim=axis, keepdim=True)
    v = x.var(dim=axis, keepdim=True, unbiased=False)
    return (x - m) / torch.sqrt(v + eps)
def _scatter_add(tgt, idx, src, axis=0):
    return tgt.clone().index_add_(axis, idx, src)
def _segment_sum(src, seg, n):
    out = torch.zeros((n,) + src.shape[1:], dtype=src.dtype)
    return out.index_add_(0, seg, src)
def _pad(x, pw):
    flat = tuple(v for pair in reversed(pw) for v in pair)
    return torch.nn.functional.pad(x, flat)

kernel_name = 'standalone__gqa_softmax__sampling'

def setup_inputs():

    _w = (lambda a: (lambda t: t.to(torch.bfloat16) if t.is_floating_point() else t)(torch.as_tensor(a)))
    B, Hq, Hkv, Sq, Sk, D = 16, 32, 8, 16, 2048, 128
    return {
        "q": _w((lambda *s: torch.randn(*s, dtype=torch.bfloat16) * 0.8)(B, Hq, Sq, D)),
        "k": _w((lambda *s: torch.randn(*s, dtype=torch.bfloat16) * 0.8)(B, Hkv, Sk, D)),
        "v": _w((lambda *s: torch.randn(*s, dtype=torch.bfloat16) * 0.05)(B, Hkv, Sk, D)),
    }

def reference(q, k, v):
    B, Hq, Sq, D = q.shape
    Hkv = k.shape[1]
    rep = Hq // Hkv
    k = (lambda x, n, axis: torch.repeat_interleave(x, n, dim=axis))(k, rep, axis=1)
    v = (lambda x, n, axis: torch.repeat_interleave(x, n, dim=axis))(v, rep, axis=1)
    scale = D ** -0.5
    scores = torch.einsum("bhqd,bhkd->bhqk", q, k) * scale
    p = _softmax(scores, axis=-1)
    return torch.einsum("bhqk,bhkd->bhqd", p, v)
```

```cpp
#include <hip/hip_runtime.h>
#include <hip/hip_bf16.h>

typedef __attribute__((ext_vector_type(8))) __bf16 bf16x8;
typedef __attribute__((ext_vector_type(4))) __bf16 bf16x4;
typedef __attribute__((ext_vector_type(2))) __bf16 bf16x2;
typedef __attribute__((ext_vector_type(4))) float f32x4;
typedef __attribute__((ext_vector_type(2))) float f32x2;
typedef __attribute__((ext_vector_type(4))) unsigned u32x4;
typedef __attribute__((ext_vector_type(2))) unsigned u32x2;
#define LAS __attribute__((address_space(3)))
#define DI __device__ __forceinline__

constexpr int B_ = 16, HQ = 32, HKV = 8, SQ = 16, SK = 2048, D = 128;
constexpr int TK = 64;
constexpr int SLOT = TK * D * 2;
constexpr int NS = 4;
constexpr int NT = SK / TK;
constexpr int VRING = NS * SLOT;
constexpr float CS = 0.088388347648318447f * 1.4426950408889634f;
constexpr float THR = 8.0f / CS;

DI float xmax(float x) {
  auto s = __builtin_amdgcn_permlane16_swap(__float_as_uint(x), __float_as_uint(x), false, false);
  x = fmaxf(__uint_as_float(s[0]), __uint_as_float(s[1]));
  auto t = __builtin_amdgcn_permlane32_swap(__float_as_uint(x), __float_as_uint(x), false, false);
  return fmaxf(__uint_as_float(t[0]), __uint_as_float(t[1]));
}
DI unsigned pk(float lo, float hi) { f32x2 v = {lo, hi}; bf16x2 b = __builtin_convertvector(v, bf16x2); return __builtin_bit_cast(unsigned, b); }
DI void bar() { asm volatile("s_waitcnt lgkmcnt(0)\n\ts_barrier" ::: "memory"); }
template <int N> DI void waitvm_bar() { asm volatile("s_waitcnt vmcnt(%0)\n\ts_barrier" :: "n"(N) : "memory"); }

DI void dma4(const char* gbase, unsigned v0, unsigned v1, unsigned v2, unsigned v3, unsigned ldsdst) {
  asm volatile("s_mov_b32 m0, %5\n\ts_nop 0\n\t"
               "global_load_lds_dwordx4 %0, %4 nt\n\t"
               "global_load_lds_dwordx4 %1, %4 offset:1024 nt\n\t"
               "global_load_lds_dwordx4 %2, %4 offset:2048 nt\n\t"
               "global_load_lds_dwordx4 %3, %4 offset:3072 nt"
               :: "v"(v0), "v"(v1), "v"(v2), "v"(v3), "s"(gbase), "s"(ldsdst) : "memory");
}
constexpr int gsz(int j) { return (j < NT ? 4 : 0) + (j >= 1 && j <= NT ? 4 : 0); }
constexpr int allowed(int it) { int last = it == 0 ? NS - 1 : it - 2 + NS; if (last > NT) last = NT; int s = 0; for (int j = it + 1; j <= last; ++j) s += gsz(j); return s; }

template <int IT> struct LoaderStep {
  static DI void run(const char* kg, const char* vg, const unsigned (&ko)[4], const unsigned (&vo)[4], unsigned ldsw) {
    waitvm_bar<allowed(IT)>();
    constexpr int j = IT - 1 + NS;
    if constexpr (IT >= 1 && j <= NT) {
      if constexpr (j < NT) dma4(kg + (size_t)j * SLOT, ko[0], ko[1], ko[2], ko[3], ldsw + (j % NS) * SLOT);
      dma4(vg + (size_t)(j - 1) * SLOT, vo[0], vo[1], vo[2], vo[3], ldsw + VRING + ((j - 1) % NS) * SLOT);
    }
    if constexpr (IT < NT) LoaderStep<IT + 1>::run(kg, vg, ko, vo, ldsw);
  }
};

DI void qk(f32x4 (&s)[4], LAS const char* const (&ka)[4], int off, const bf16x8 (&qf)[4]) {
#pragma unroll
  for (int kb = 0; kb < 4; ++kb) {
    f32x4 a = {0.f, 0.f, 0.f, 0.f};
#pragma unroll
    for (int st = 0; st < 4; ++st) {
      bf16x8 kf = *(LAS const bf16x8*)(ka[st] + off + kb * 4096);
      a = __builtin_amdgcn_mfma_f32_16x16x32_bf16(kf, qf[st], a, 0, 0, 0);
    }
    s[kb] = a;
  }
}
DI void pv(f32x4 (&o)[8], f32x4& l, LAS const char* const (&va)[8], int off, const bf16x8 (&p)[2], bf16x8 ones) {
#pragma unroll
  for (int ks = 0; ks < 2; ++ks) {
#pragma unroll
    for (int c = 0; c < 8; ++c) {
      bf16x4 lo = __builtin_amdgcn_ds_read_tr16_b64_v4bf16((LAS bf16x4*)(va[c] + off + ks * 8192));
      bf16x4 hi = __builtin_amdgcn_ds_read_tr16_b64_v4bf16((LAS bf16x4*)(va[c] + off + ks * 8192 + 4096));
      bf16x8 vf = __builtin_shufflevector(lo, hi, 0, 1, 2, 3, 4, 5, 6, 7);
      o[c] = __builtin_amdgcn_mfma_f32_16x16x32_bf16(vf, p[ks], o[c], 0, 0, 0);
    }
    l = __builtin_amdgcn_mfma_f32_16x16x32_bf16(ones, p[ks], l, 0, 0, 0);
  }
}
DI float tilemax(const f32x4 (&s)[4]) {
  float m0 = fmaxf(fmaxf(s[0][0], s[0][1]), fmaxf(s[0][2], s[0][3]));
  float m1 = fmaxf(fmaxf(s[1][0], s[1][1]), fmaxf(s[1][2], s[1][3]));
  float m2 = fmaxf(fmaxf(s[2][0], s[2][1]), fmaxf(s[2][2], s[2][3]));
  float m3 = fmaxf(fmaxf(s[3][0], s[3][1]), fmaxf(s[3][2], s[3][3]));
  return xmax(fmaxf(fmaxf(m0, m1), fmaxf(m2, m3)));
}
DI void expo(bf16x8 (&p)[2], const f32x4 (&s)[4], float m) {
  const float nm = -m * CS;
#pragma unroll
  for (int ks = 0; ks < 2; ++ks) {
    float e[8];
#pragma unroll
    for (int j = 0; j < 8; ++j) e[j] = __builtin_amdgcn_exp2f(fmaf(s[2 * ks + (j >> 2)][j & 3], CS, nm));
    u32x4 w = {pk(e[0], e[1]), pk(e[2], e[3]), pk(e[4], e[5]), pk(e[6], e[7])};
    p[ks] = __builtin_bit_cast(bf16x8, w);
  }
}

__global__ __launch_bounds__(512, 2) void gqa_kernel(const __hip_bfloat16* __restrict__ Q, const __hip_bfloat16* __restrict__ K, const __hip_bfloat16* __restrict__ V, __hip_bfloat16* __restrict__ O) {
  __shared__ __attribute__((aligned(16))) char lds[2 * NS * SLOT];
  const int tid = threadIdx.x, lane = tid & 63;
  const int wave = __builtin_amdgcn_readfirstlane(tid >> 6);
  const int bid = blockIdx.x;
  const int bh = (bid & 7) * 16 + (bid >> 3);
  const unsigned ldsb = (unsigned)(size_t)(LAS char*)lds;
  const int g = lane >> 4, r = lane & 15;
  if (wave >= 4) {
    const int w = wave - 4;
    const char* kg = (const char*)K + (size_t)bh * SK * D * 2 + w * 4096;
    const char* vg = (const char*)V + (size_t)bh * SK * D * 2 + w * 4096;
    unsigned ko[4], vo[4];
#pragma unroll
    for (int j = 0; j < 4; ++j) { const int row = 4 * j + g; ko[j] = g * 256 + 16 * (r ^ row); vo[j] = g * 256 + 16 * (r ^ (2 * (row & 7))); }
    const unsigned ldsw = ldsb + w * 4096;
#pragma unroll
    for (int j = 0; j < NS; ++j) {
      dma4(kg + (size_t)j * SLOT, ko[0], ko[1], ko[2], ko[3], ldsw + j * SLOT);
      if (j >= 1) dma4(vg + (size_t)(j - 1) * SLOT, vo[0], vo[1], vo[2], vo[3], ldsw + VRING + (j - 1) * SLOT);
    }
    LoaderStep<0>::run(kg, vg, ko, vo, ldsw);
  } else {
    const __hip_bfloat16* qrow = Q + ((size_t)(bh * 4 + wave) * SQ + r) * D + 8 * g;
    bf16x8 qf[4];
#pragma unroll
    for (int st = 0; st < 4; ++st) qf[st] = *(const bf16x8*)(qrow + 32 * st);
    LAS const char* ka[4]; LAS const char* va[8];
#pragma unroll
    for (int st = 0; st < 4; ++st) ka[st] = (LAS const char*)lds + 256 * r + 16 * ((4 * st + g) ^ r);
    const int qq = r >> 2, pp = r & 3, x = 4 * (g & 1) + qq;
#pragma unroll
    for (int c = 0; c < 8; ++c) va[c] = (LAS const char*)lds + VRING + 256 * (4 * g + qq) + 32 * (c ^ x) + 8 * pp;
    const u32x4 onesw = {0x3f803f80u, 0x3f803f80u, 0x3f803f80u, 0x3f803f80u};
    const bf16x8 ones = __builtin_bit_cast(bf16x8, onesw);
    f32x4 o[8], l = {0.f, 0.f, 0.f, 0.f};
#pragma unroll
    for (int c = 0; c < 8; ++c) o[c] = f32x4{0.f, 0.f, 0.f, 0.f};
    f32x4 s[4]; bf16x8 p[2];
    bar();
    qk(s, ka, 0, qf);
    float m = tilemax(s);
    expo(p, s, m);
#pragma unroll 1
    for (int t0 = 1; t0 < NT + 1; t0 += NS) {
#pragma unroll
      for (int u = 0; u < NS; ++u) {
        const int t = t0 + u;
        bar();
        if (u == NS - 1 && t == NT) { pv(o, l, va, u * SLOT, p, ones); break; }
        qk(s, ka, ((1 + u) % NS) * SLOT, qf);
        pv(o, l, va, u * SLOT, p, ones);
        const float tm = tilemax(s);
        const bool need = tm > m + THR;
        const float mn = need ? tm : m;
        const float alpha = __builtin_amdgcn_exp2f((m - mn) * CS);
        m = mn;
        expo(p, s, m);
        if (__any(need)) {
#pragma unroll
          for (int c = 0; c < 8; ++c) o[c] *= alpha;
          l *= alpha;
        }
      }
    }
    const float inv = 1.0f / l[0];
    __hip_bfloat16* orow = O + ((size_t)(bh * 4 + wave) * SQ + r) * D + 4 * g;
#pragma unroll
    for (int c = 0; c < 8; ++c) {
      u32x2 w = {pk(o[c][0] * inv, o[c][1] * inv), pk(o[c][2] * inv, o[c][3] * inv)};
      *(u32x2*)(orow + 16 * c) = w;
    }
  }
}

extern "C" void kernel_launch(void* const* d_in, const int* in_sizes, int n_in, void* d_out, int out_size, void* d_ws, size_t ws_size, hipStream_t stream) {
  gqa_kernel<<<B_ * HKV, 512, 0, stream>>>((const __hip_bfloat16*)d_in[0], (const __hip_bfloat16*)d_in[1], (const __hip_bfloat16*)d_in[2], (__hip_bfloat16*)d_out);
}
```

```cpp
#include <hip/hip_runtime.h>
#include <hip/hip_bf16.h>

typedef __attribute__((ext_vector_type(8))) __bf16 bf16x8;
typedef __attribute__((ext_vector_type(4))) __bf16 bf16x4;
typedef __attribute__((ext_vector_type(2))) __bf16 bf16x2;
typedef __attribute__((ext_vector_type(4))) float f32x4;
typedef __attribute__((ext_vector_type(2))) float f32x2;
typedef __attribute__((ext_vector_type(4))) unsigned u32x4;
typedef __attribute__((ext_vector_type(2))) unsigned u32x2;
#define LAS __attribute__((address_space(3)))
#define DI __device__ __forceinline__

constexpr int B_ = 16, HQ = 32, HKV = 8, SQ = 16, SK = 2048, D = 128;
constexpr int TK = 64;
constexpr int SLOT = TK * D * 2;
constexpr int NS = 4;
constexpr int NT = SK / TK;
constexpr int VRING = NS * SLOT;
constexpr float CS = 0.088388347648318447f * 1.4426950408889634f;
constexpr float THR = 8.0f / CS;

DI float xmax(float x) {
  auto s = __builtin_amdgcn_permlane16_swap(__float_as_uint(x), __float_as_uint(x), false, false);
  x = fmaxf(__uint_as_float(s[0]), __uint_as_float(s[1]));
  auto t = __builtin_amdgcn_permlane32_swap(__float_as_uint(x), __float_as_uint(x), false, false);
  return fmaxf(__uint_as_float(t[0]), __uint_as_float(t[1]));
}
DI unsigned pk(float lo, float hi) { f32x2 v = {lo, hi}; bf16x2 b = __builtin_convertvector(v, bf16x2); return __builtin_bit_cast(unsigned, b); }
DI void bar() { asm volatile("s_waitcnt lgkmcnt(0)\n\ts_barrier" ::: "memory"); }
template <int N> DI void waitvm_bar() { asm volatile("s_waitcnt vmcnt(%0)\n\ts_barrier" :: "n"(N) : "memory"); }

DI void dma4(const char* gbase, unsigned v0, unsigned v1, unsigned v2, unsigned v3, unsigned ldsdst) {
  asm volatile("s_mov_b32 m0, %5\n\ts_nop 0\n\t"
               "global_load_lds_dwordx4 %0, %4 nt\n\t"
               "global_load_lds_dwordx4 %1, %4 offset:1024 nt\n\t"
               "global_load_lds_dwordx4 %2, %4 offset:2048 nt\n\t"
               "global_load_lds_dwordx4 %3, %4 offset:3072 nt"
               :: "v"(v0), "v"(v1), "v"(v2), "v"(v3), "s"(gbase), "s"(ldsdst) : "memory");
}
constexpr int gsz(int j) { return (j < NT ? 4 : 0) + (j >= 1 && j <= NT ? 4 : 0); }
constexpr int allowed(int it) { int last = it == 0 ? NS - 1 : it - 2 + NS; if (last > NT) last = NT; int s = 0; for (int j = it + 1; j <= last; ++j) s += gsz(j); return s; }

template <int IT> struct LoaderStep {
  static DI void run(const char* kg, const char* vg, const unsigned (&ko)[4], const unsigned (&vo)[4], unsigned ldsw) {
    waitvm_bar<allowed(IT)>();
    constexpr int j = IT - 1 + NS;
    if constexpr (IT >= 1 && j <= NT) {
      if constexpr (j < NT) dma4(kg + (size_t)j * SLOT, ko[0], ko[1], ko[2], ko[3], ldsw + (j % NS) * SLOT);
      dma4(vg + (size_t)(j - 1) * SLOT, vo[0], vo[1], vo[2], vo[3], ldsw + VRING + ((j - 1) % NS) * SLOT);
    }
    if constexpr (IT < NT) LoaderStep<IT + 1>::run(kg, vg, ko, vo, ldsw);
  }
};

DI void qk(f32x4 (&s)[4], LAS const char* const (&ka)[4], int off, const bf16x8 (&qf)[4]) {
#pragma unroll
  for (int kb = 0; kb < 4; ++kb) {
    f32x4 a = {0.f, 0.f, 0.f, 0.f};
#pragma unroll
    for (int st = 0; st < 4; ++st) {
      bf16x8 kf = *(LAS const bf16x8*)(ka[st] + off + kb * 4096);
      a = __builtin_amdgcn_mfma_f32_16x16x32_bf16(kf, qf[st], a, 0, 0, 0);
    }
    s[kb] = a;
  }
}
DI void pv(f32x4 (&o)[8], f32x4& l, LAS const char* const (&va)[8], int off, const bf16x8 (&p)[2], bf16x8 ones) {
#pragma unroll
  for (int ks = 0; ks < 2; ++ks) {
#pragma unroll
    for (int c = 0; c < 8; ++c) {
      bf16x4 lo = __builtin_amdgcn_ds_read_tr16_b64_v4bf16((LAS bf16x4*)(va[c] + off + ks * 8192));
      bf16x4 hi = __builtin_amdgcn_ds_read_tr16_b64_v4bf16((LAS bf16x4*)(va[c] + off + ks * 8192 + 4096));
      bf16x8 vf = __builtin_shufflevector(lo, hi, 0, 1, 2, 3, 4, 5, 6, 7);
      o[c] = __builtin_amdgcn_mfma_f32_16x16x32_bf16(vf, p[ks], o[c], 0, 0, 0);
    }
    l = __builtin_amdgcn_mfma_f32_16x16x32_bf16(ones, p[ks], l, 0, 0, 0);
  }
}
DI float tilemax(const f32x4 (&s)[4]) {
  float m0 = fmaxf(fmaxf(s[0][0], s[0][1]), fmaxf(s[0][2], s[0][3]));
  float m1 = fmaxf(fmaxf(s[1][0], s[1][1]), fmaxf(s[1][2], s[1][3]));
  float m2 = fmaxf(fmaxf(s[2][0], s[2][1]), fmaxf(s[2][2], s[2][3]));
  float m3 = fmaxf(fmaxf(s[3][0], s[3][1]), fmaxf(s[3][2], s[3][3]));
  return xmax(fmaxf(fmaxf(m0, m1), fmaxf(m2, m3)));
}
DI void expo(bf16x8 (&p)[2], const f32x4 (&s)[4], float m) {
  const float nm = -m * CS;
#pragma unroll
  for (int ks = 0; ks < 2; ++ks) {
    float e[8];
#pragma unroll
    for (int j = 0; j < 8; ++j) e[j] = __builtin_amdgcn_exp2f(fmaf(s[2 * ks + (j >> 2)][j & 3], CS, nm));
    u32x4 w = {pk(e[0], e[1]), pk(e[2], e[3]), pk(e[4], e[5]), pk(e[6], e[7])};
    p[ks] = __builtin_bit_cast(bf16x8, w);
  }
}

__global__ __launch_bounds__(512, 2) void gqa_kernel(const __hip_bfloat16* __restrict__ Q, const __hip_bfloat16* __restrict__ K, const __hip_bfloat16* __restrict__ V, __hip_bfloat16* __restrict__ O) {
  __shared__ __attribute__((aligned(16))) char lds[2 * NS * SLOT];
  const unsigned long long tstart = __builtin_amdgcn_s_memrealtime();
  unsigned long long tm1 = 0;
  const int tid = threadIdx.x, lane = tid & 63;
  const int wave = __builtin_amdgcn_readfirstlane(tid >> 6);
  const int bid = blockIdx.x;
  const int bh = (bid & 7) * 16 + (bid >> 3);
  const unsigned ldsb = (unsigned)(size_t)(LAS char*)lds;
  const int g = lane >> 4, r = lane & 15;
  if (wave >= 4) {
    const int w = wave - 4;
    const char* kg = (const char*)K + (size_t)bh * SK * D * 2 + w * 4096;
    const char* vg = (const char*)V + (size_t)bh * SK * D * 2 + w * 4096;
    unsigned ko[4], vo[4];
#pragma unroll
    for (int j = 0; j < 4; ++j) { const int row = 4 * j + g; ko[j] = g * 256 + 16 * (r ^ row); vo[j] = g * 256 + 16 * (r ^ (2 * (row & 7))); }
    const unsigned ldsw = ldsb + w * 4096;
#pragma unroll
    for (int j = 0; j < NS; ++j) {
      dma4(kg + (size_t)j * SLOT, ko[0], ko[1], ko[2], ko[3], ldsw + j * SLOT);
      if (j >= 1) dma4(vg + (size_t)(j - 1) * SLOT, vo[0], vo[1], vo[2], vo[3], ldsw + VRING + (j - 1) * SLOT);
    }
    LoaderStep<0>::run(kg, vg, ko, vo, ldsw);
  } else {
    const __hip_bfloat16* qrow = Q + ((size_t)(bh * 4 + wave) * SQ + r) * D + 8 * g;
    asm volatile("" :: "s"(Q), "s"(O) : "memory");
    __builtin_amdgcn_sched_barrier(0); asm volatile("s_memrealtime %0\n\ts_waitcnt lgkmcnt(0)" : "=s"(tm1) :: "memory"); __builtin_amdgcn_sched_barrier(0);
    bf16x8 qf[4];
#pragma unroll
    for (int st = 0; st < 4; ++st) qf[st] = *(const bf16x8*)(qrow + 32 * st);
    LAS const char* ka[4]; LAS const char* va[8];
#pragma unroll
    for (int st = 0; st < 4; ++st) ka[st] = (LAS const char*)lds + 256 * r + 16 * ((4 * st + g) ^ r);
    const int qq = r >> 2, pp = r & 3, x = 4 * (g & 1) + qq;
#pragma unroll
    for (int c = 0; c < 8; ++c) va[c] = (LAS const char*)lds + VRING + 256 * (4 * g + qq) + 32 * (c ^ x) + 8 * pp;
    const u32x4 onesw = {0x3f803f80u, 0x3f803f80u, 0x3f803f80u, 0x3f803f80u};
    const bf16x8 ones = __builtin_bit_cast(bf16x8, onesw);
    f32x4 o[8], l = {0.f, 0.f, 0.f, 0.f};
#pragma unroll
    for (int c = 0; c < 8; ++c) o[c] = f32x4{0.f, 0.f, 0.f, 0.f};
    f32x4 s[4]; bf16x8 p[2];
    bar();
    qk(s, ka, 0, qf);
    float m = tilemax(s);
    expo(p, s, m);
#pragma unroll 1
    for (int t0 = 1; t0 < NT + 1; t0 += NS) {
#pragma unroll
      for (int u = 0; u < NS; ++u) {
        const int t = t0 + u;
        bar();
        if (u == NS - 1 && t == NT) { pv(o, l, va, u * SLOT, p, ones); break; }
        qk(s, ka, ((1 + u) % NS) * SLOT, qf);
        pv(o, l, va, u * SLOT, p, ones);
        const float tm = tilemax(s);
        const bool need = tm > m + THR;
        const float mn = need ? tm : m;
        const float alpha = __builtin_amdgcn_exp2f((m - mn) * CS);
        m = mn;
        expo(p, s, m);
        if (__any(need)) {
#pragma unroll
          for (int c = 0; c < 8; ++c) o[c] *= alpha;
          l *= alpha;
        }
      }
    }
    const float inv = 1.0f / l[0];
    __hip_bfloat16* orow = O + ((size_t)(bh * 4 + wave) * SQ + r) * D + 4 * g;
#pragma unroll
    for (int c = 0; c < 8; ++c) {
      u32x2 w = {pk(o[c][0] * inv, o[c][1] * inv), pk(o[c][2] * inv, o[c][3] * inv)};
      *(u32x2*)(orow + 16 * c) = w;
    }
    asm volatile("s_waitcnt vmcnt(0)" ::: "memory");
    while (__builtin_amdgcn_s_memrealtime() - tstart < 5000ull + 4ull * (tm1 - tstart)) __builtin_amdgcn_s_sleep(1);
  }
}

extern "C" void kernel_launch(void* const* d_in, const int* in_sizes, int n_in, void* d_out, int out_size, void* d_ws, size_t ws_size, hipStream_t stream) {
  gqa_kernel<<<B_ * HKV, 512, 0, stream>>>((const __hip_bfloat16*)d_in[0], (const __hip_bfloat16*)d_in[1], (const __hip_bfloat16*)d_in[2], (__hip_bfloat16*)d_out);
}
```

```cpp
#include <hip/hip_runtime.h>
#include <hip/hip_bf16.h>

__global__ __launch_bounds__(512, 2) void gqa_kernel(const __hip_bfloat16* __restrict__ Q, const __hip_bfloat16* __restrict__ K,
                                                     const __hip_bfloat16* __restrict__ V, __hip_bfloat16* __restrict__ O, char* __restrict__ ws) {
  __shared__ __attribute__((aligned(16))) char lds[9 * 16384];
  const unsigned ldsb = (unsigned)(size_t)(__attribute__((address_space(3))) char*)lds;
  asm volatile(
      "s_load_dwordx8 s[20:27], %0, 0x0\n\t"
      "s_load_dwordx2 s[28:29], %0, 0x20\n\t"
      "v_lshrrev_b32 v10, 6, %2\n\t"
      "v_and_b32 v1, 63, %2\n\t"
      "v_readfirstlane_b32 s38, v10\n\t"
      "s_and_b32 s52, %1, 7\n\t"
      "s_lshl_b32 s52, s52, 4\n\t"
      "s_lshr_b32 s54, %1, 4\n\t"
      "s_add_u32 s54, s54, s52\n\t"
      "s_bfe_u32 s53, %1, 0x10003\n\t"
      "s_cmp_lt_u32 s38, 4\n\t"
      "s_cbranch_scc1 L_LOAD%=\n\t"
      "s_mov_b64 exec, 0\n\t"
      "s_cmp_eq_u32 s38, 5\n\t"
      "s_cbranch_scc1 L_PADqk%=\n\t"
      "s_cmp_eq_u32 s38, 6\n\t"
      "s_cbranch_scc1 L_PADrare%=\n\t"
      "s_cmp_eq_u32 s38, 7\n\t"
      "s_cbranch_scc1 L_PADevt%=\n\t"
      "s_getpc_b64 s[30:31]\n\t"
      "s_bfm_b64 exec, 59, 0\n\t"
      "v_lshlrev_b32 v10, 6, v1\n\t"
      "global_load_dword v126, v10, s[30:31]\n\t"
      "s_branch L_CONS%=\n\t"
      "L_LOAD%=:\n\t"
      "v_bfe_u32 v7, v1, 2, 3\n\t"
      "s_lshr_b32 s52, s38, 1\n\t"
      "s_lshl_b32 s52, s52, 3\n\t"
      "v_add_u32 v10, s52, v7\n\t"
      "s_and_b32 s52, s38, 1\n\t"
      "s_lshl_b32 s52, s52, 7\n\t"
      "v_and_b32 v12, 32, v1\n\t"
      "v_lshlrev_b32 v33, 1, v12\n\t"
      "v_lshl_add_u32 v12, v10, 8, v33\n\t"
      "v_add_u32 v12, s52, v12\n\t"
      "v_lshrrev_b32 v10, 2, v10\n\t"
      "v_sub_u32 v13, 0, v10\n\t"
      "v_xor_b32 v13, v13, v1\n\t"
      "v_and_b32 v13, 3, v13\n\t"
      "v_lshl_add_u32 v32, v13, 4, v12\n\t"
      "s_movk_i32 s41, 0x1000\n\t"
      "s_movk_i32 s50, 0x1000\n\t"
      "s_lshl_b32 s42, s38, 10\n\t"
      "s_add_u32 s42, s42, %3\n\t"
      "s_lshl_b32 s52, s54, 1\n\t"
      "s_add_u32 s52, s52, s53\n\t"
      "s_lshl_b32 s52, s52, 18\n\t"
      "s_lshl_b32 s43, s54, 14\n\t"
      "s_mov_b32 s44, 0\n\t"
      "s_mov_b32 s49, 0\n\t"
      "s_mov_b32 s46, 0\n\t"
      "s_add_u32 m0, s42, 131072\n\t"
      "s_waitcnt lgkmcnt(0)\n\t"
      "s_memrealtime s[34:35]\n\t"
      "s_add_u32 s20, s20, s43\n\t"
      "s_addc_u32 s21, s21, 0\n\t"
      "global_load_lds_dwordx4 v32, s[20:21]\n\t"
      "s_add_u32 m0, m0, s41\n\t"
      "s_add_u32 s20, s20, s41\n\t"
      "s_addc_u32 s21, s21, 0\n\t"
      "global_load_lds_dwordx4 v32, s[20:21]\n\t"
      "s_add_u32 m0, m0, s41\n\t"
      "s_add_u32 s20, s20, s41\n\t"
      "s_addc_u32 s21, s21, 0\n\t"
      "global_load_lds_dwordx4 v32, s[20:21]\n\t"
      "s_add_u32 m0, m0, s41\n\t"
      "s_add_u32 s20, s20, s41\n\t"
      "s_addc_u32 s21, s21, 0\n\t"
      "global_load_lds_dwordx4 v32, s[20:21]\n\t"
      "s_add_u32 s22, s22, s52\n\t"
      "s_addc_u32 s23, s23, 0\n\t"
      "s_add_u32 s24, s24, s52\n\t"
      "s_addc_u32 s25, s25, 0\n\t"
      "s_waitcnt lgkmcnt(0)\n\t"
      "s_lshl_b32 s48, s34, 2\n\t"
      "s_add_u32 s48, s48, -16\n\t"
      "s_mov_b32 s43, 6\n\t"
      "s_mov_b32 s51, 6\n\t"
      "L_IS%=:\n\t"
      "s_add_u32 m0, s42, s49\n\t"
      "s_add_u32 s49, s49, s41\n\t"
      "global_load_lds_dwordx4 v32, s[22:23] nt\n\t"
      "s_and_b32 s49, s49, 0x1ffff\n\t"
      "s_add_u32 s22, s22, s50\n\t"
      "s_addc_u32 s23, s23, 0\n\t"
      "s_add_u32 s44, s44, 1\n\t"
      "s_and_b32 s52, s44, 0x5f\n\t"
      "s_cmp_eq_u32 s52, 64\n\t"
      "s_cbranch_scc1 L_SW%=\n\t"
      "L_IC%=:\n\t"
      "s_cmp_lt_u32 s44, s43\n\t"
      "s_cbranch_scc1 L_IS%=\n\t"
      "s_cmp_lg_u32 s46, 0\n\t"
      "s_cbranch_scc1 L_W%=\n\t"
      "s_getpc_b64 s[30:31]\n\t"
      "L_PC%=:\n\t"
      "s_add_u32 s30, s30, L_TAB%=-L_PC%=\n\t"
      "s_addc_u32 s31, s31, 0\n\t"
      "s_lshl_b32 s52, s38, 3\n\t"
      "v_add_u32 v10, s52, v7\n\t"
      "v_lshl_add_u32 v12, v10, 8, v33\n\t"
      "v_lshrrev_b32 v10, 2, v10\n\t"
      "v_sub_u32 v13, 0, v10\n\t"
      "v_xor_b32 v13, v13, v1\n\t"
      "v_and_b32 v13, 3, v13\n\t"
      "v_lshl_add_u32 v12, v13, 4, v12\n\t"
      "s_lshl_b32 s52, s53, 7\n\t"
      "v_add_u32 v33, s52, v12\n\t"
      "s_mov_b64 exec, 0\n\t"
      "s_cmp_eq_u32 s38, 0\n\t"
      "s_cbranch_scc1 L_PADv%=\n\t"
      "s_cmp_eq_u32 s38, 1\n\t"
      "s_cbranch_scc1 L_PADst%=\n\t"
      "s_cmp_eq_u32 s38, 2\n\t"
      "s_cbranch_scc1 L_PADpre%=\n\t"
      "s_branch L_PADfin%=\n\t"
      "L_LWARM%=:\n\t"
      "s_mov_b64 exec, -1\n\t"
      "L_W%=:\n\t"
      "s_add_u32 s46, s46, 2\n\t"
      "s_sub_u32 s52, s44, s46\n\t"
      "s_lshl_b32 s52, s52, 3\n\t"
      "s_add_u32 s18, s30, s52\n\t"
      "s_addc_u32 s19, s31, 0\n\t"
      "s_setpc_b64 s[18:19]\n\t"
      "L_TAB%=:\n\t"
      "s_waitcnt vmcnt(0)\n\t"
      "s_branch L_B%=\n\t"
      "s_waitcnt vmcnt(1)\n\t"
      "s_branch L_B%=\n\t"
      "s_waitcnt vmcnt(2)\n\t"
      "s_branch L_B%=\n\t"
      "s_waitcnt vmcnt(3)\n\t"
      "s_branch L_B%=\n\t"
      "s_waitcnt vmcnt(4)\n\t"
      "s_branch L_B%=\n\t"
      "s_waitcnt vmcnt(5)\n\t"
      "s_branch L_B%=\n\t"
      "s_waitcnt vmcnt(6)\n\t"
      "s_branch L_B%=\n\t"
      "s_waitcnt vmcnt(7)\n\t"
      "s_branch L_B%=\n\t"
      "s_waitcnt vmcnt(8)\n\t"
      "s_branch L_B%=\n\t"
      "s_waitcnt vmcnt(9)\n\t"
      "s_branch L_B%=\n\t"
      "s_waitcnt vmcnt(10)\n\t"
      "s_branch L_B%=\n\t"
      "L_B%=:\n\t"
      "s_and_b32 s52, s46, 0xc2\n\t"
      "s_cmp_eq_u32 s52, 2\n\t"
      "s_cbranch_scc1 L_RF%=\n\t"
      "s_memrealtime s[34:35]\n\t"
      "s_barrier\n\t"
      "s_cmp_eq_u32 s46, 128\n\t"
      "s_cbranch_scc1 L_LX%=\n\t"
      "s_cmp_gt_u32 s46, 96\n\t"
      "s_cselect_b32 s51, 4, 0\n\t"
      "s_cmp_eq_u32 s46, 102\n\t"
      "s_cselect_b32 s51, 124, s51\n\t"
      "s_add_u32 s48, s48, s51\n\t"
      "s_mul_i32 s51, s46, 56\n\t"
      "s_add_u32 s51, s51, s48\n\t"
      "s_waitcnt lgkmcnt(0)\n\t"
      "s_lshl_b32 s52, s34, 2\n\t"
      "s_sub_i32 s52, s52, s51\n\t"
      "s_ashr_i32 s52, s52, 7\n\t"
      "s_max_i32 s52, s52, 0\n\t"
      "s_min_i32 s52, s52, 5\n\t"
      "s_add_u32 s51, s52, 5\n\t"
      "s_cmp_ge_u32 s46, 118\n\t"
      "s_cselect_b32 s51, 10, s51\n\t"
      "s_and_b32 s52, s46, 0x5f\n\t"
      "s_cmp_eq_u32 s52, 64\n\t"
      "s_cselect_b32 s52, 2, 0\n\t"
      "s_add_u32 s51, s51, s52\n\t"
      "L_RF%=:\n\t"
      "s_add_u32 s43, s46, s51\n\t"
      "s_min_u32 s43, s43, 128\n\t"
      "s_branch L_IC%=\n\t"
      "L_SW%=:\n\t"
      "v_xor_b32 v33, 0x80, v33\n\t"
      "s_mov_b64 s[22:23], s[24:25]\n\t"
      "s_movk_i32 s50, 0x2000\n\t"
      "v_mov_b32 v32, v33\n\t"
      "s_branch L_IC%=\n\t"
      "L_LX%=:\n\t"
      "s_endpgm\n\t"
      "L_CONS%=:\n\t"
      "s_mov_b64 exec, -1\n\t"
      "s_sub_u32 s38, s38, 4\n\t"
      "s_lshl_b32 s52, s54, 2\n\t"
      "s_add_u32 s52, s52, s38\n\t"
      "s_lshl_b32 s47, s52, 1\n\t"
      "s_add_u32 s47, s47, s53\n\t"
      "s_xor_b32 s48, s47, 1\n\t"
      "s_mul_i32 s46, s48, 2560\n\t"
      "s_add_u32 s46, s46, 131072\n\t"
      "s_mul_i32 s51, s47, 2560\n\t"
      "s_add_u32 s51, s51, 131072\n\t"
      "s_lshl_b32 s47, s47, 7\n\t"
      "s_lshl_b32 s48, s48, 7\n\t"
      "s_lshl_b32 s45, s52, 5\n\t"
      "s_add_u32 s45, s45, s53\n\t"
      "s_lshl_b32 s45, s45, 7\n\t"
      "v_mov_b32 v9, 0\n\t"
      "v_lshrrev_b32 v10, 4, v1\n\t"
      "v_and_b32 v12, 15, v1\n\t"
      "v_lshrrev_b32 v13, 2, v12\n\t"
      "v_sub_u32 v7, 0, v13\n\t"
      "v_and_b32 v7, 3, v7\n\t"
      "v_xor_b32 v7, v7, v10\n\t"
      "v_lshlrev_b32 v2, 4, v7\n\t"
      "v_and_b32 v7, 7, v12\n\t"
      "v_lshl_add_u32 v2, v7, 6, v2\n\t"
      "v_lshlrev_b32 v4, 8, v7\n\t"
      "v_lshrrev_b32 v7, 3, v12\n\t"
      "v_lshl_add_u32 v2, v7, 11, v2\n\t"
      "v_add_u32 v2, %3, v2\n\t"
      "v_lshl_add_u32 v4, v7, 6, v4\n\t"
      "v_and_b32 v7, 3, v1\n\t"
      "v_lshlrev_b32 v3, 3, v7\n\t"
      "v_sub_u32 v7, 0, v10\n\t"
      "v_and_b32 v7, 3, v7\n\t"
      "v_lshlrev_b32 v7, 4, v7\n\t"
      "v_xor_b32 v3, v3, v7\n\t"
      "v_lshl_add_u32 v3, v13, 6, v3\n\t"
      "v_and_b32 v7, 1, v10\n\t"
      "v_lshl_add_u32 v3, v7, 8, v3\n\t"
      "v_lshl_add_u32 v4, v7, 5, v4\n\t"
      "v_add_u32 v3, %3, v3\n\t"
      "v_lshrrev_b32 v7, 1, v10\n\t"
      "v_lshl_add_u32 v3, v7, 10, v3\n\t"
      "v_lshl_add_u32 v4, v7, 4, v4\n\t"
      "v_lshlrev_b32 v5, 4, v1\n\t"
      "v_lshlrev_b32 v11, 4, v12\n\t"
      "v_add_u32 v11, 0x800, v11\n\t"
      "s_lshl_b32 s52, s38, 12\n\t"
      "s_add_u32 s52, s52, 131072\n\t"
      "v_add_u32 v10, s52, v2\n\t"
      "v_mov_b32 v6, v2\n\t"
      "s_mov_b32 s49, 0x40004000\n\t"
      "v_mov_b32 v20, 0\n\t"
      "v_mov_b32 v21, 0\n\t"
      "v_mov_b64 v[22:23], v[20:21]\n\t"
      "v_mov_b64 v[24:25], v[20:21]\n\t"
      "v_mov_b64 v[26:27], v[20:21]\n\t"
      "v_mov_b64 v[28:29], v[20:21]\n\t"
      "v_mov_b64 v[30:31], v[20:21]\n\t"
      "v_mov_b32 v16, 0x3f803f80\n\t"
      "v_mov_b32 v17, v16\n\t"
      "v_mov_b64 v[18:19], v[16:17]\n\t"
      "v_mov_b32 v8, 0x7f800000\n\t"
      "s_mov_b32 s40, 0\n\t"
      "s_mov_b32 s42, 3\n\t"
      "s_mov_b32 s44, 0\n\t"
      "s_mov_b32 s41, 1\n\t"
      "s_mov_b32 s39, 0\n\t"
      "s_cmp_eq_u32 s38, 0\n\t"
      "s_cselect_b32 s41, s41, 16\n\t"
      "s_waitcnt lgkmcnt(0)\n\t"
      "s_add_u32 s30, s28, s47\n\t"
      "s_addc_u32 s31, s29, 0\n\t"
      "s_add_u32 s18, s28, s48\n\t"
      "s_addc_u32 s19, s29, 0\n\t"
      "s_add_u32 s34, s28, s46\n\t"
      "s_addc_u32 s35, s29, 0\n\t"
      "s_add_u32 s16, s28, s51\n\t"
      "s_addc_u32 s17, s29, 0\n\t"
      "s_add_u32 s26, s26, s45\n\t"
      "s_addc_u32 s27, s27, 0\n\t"
      "s_lshl_b32 s52, s54, 1\n\t"
      "s_add_u32 s52, s52, s53\n\t"
      "s_lshl_b32 s52, s52, 18\n\t"
      "s_add_u32 s24, s24, s52\n\t"
      "s_addc_u32 s25, s25, 0\n\t"
      "s_cmp_eq_u32 s38, 1\n\t"
      "s_cselect_b64 s[24:25], s[16:17], s[24:25]\n\t"
      "s_cmp_eq_u32 s38, 2\n\t"
      "s_cselect_b64 s[24:25], s[34:35], s[24:25]\n\t"
      "s_cmp_eq_u32 s38, 3\n\t"
      "s_cselect_b64 s[24:25], s[26:27], s[24:25]\n\t"
      "s_mov_b64 exec, 1\n\t"
      "global_load_dword v127, v9, s[24:25] offset:2304 nt\n\t"
      "s_mov_b64 exec, -1\n\t"
      "s_barrier\n\t"
      "ds_read_b128 v[32:35], v10 offset:0\n\t"
      "ds_read_b128 v[36:39], v10 offset:512\n\t"
      "ds_read_b128 v[40:43], v10 offset:1024\n\t"
      "ds_read_b128 v[44:47], v10 offset:1536\n\t"
      "s_branch L_QKB%=\n\t"
      "L_PADqk%=:\n\t"
      "s_cbranch_execz L_CONS%=\n\t"
      "L_QK%=:\n\t"
      "s_barrier\n\t"
      "L_QKB%=:\n\t"
      "ds_read_b128 v[80:83], v6 offset:0\n\t"
      "ds_read_b128 v[84:87], v6 offset:512\n\t"
      "ds_read_b128 v[88:91], v6 offset:1024\n\t"
      "ds_read_b128 v[92:95], v6 offset:1536\n\t"
      "ds_read_b128 v[96:99], v6 offset:4096\n\t"
      "ds_read_b128 v[100:103], v6 offset:4608\n\t"
      "ds_read_b128 v[104:107], v6 offset:5120\n\t"
      "ds_read_b128 v[108:111], v6 offset:5632\n\t"
      "ds_read_b128 v[112:115], v6 offset:8192\n\t"
      "ds_read_b128 v[116:119], v6 offset:8704\n\t"
      "v_mfma_f32_16x16x32_bf16 v[20:23], v[16:19], v[24:27], v[20:23]\n\t"
      "v_mfma_f32_16x16x32_bf16 v[20:23], v[16:19], v[28:31], v[20:23]\n\t"
      "s_add_u32 s40, s40, 1\n\t"
      "s_add_u32 s44, s44, 0x4000\n\t"
      "s_and_b32 s44, s44, 0x1c000\n\t"
      "s_waitcnt lgkmcnt(9)\n\t"
      "v_mfma_f32_16x16x32_bf16 v[64:67], v[80:83], v[32:35], 0\n\t"
      "ds_read_b128 v[120:123], v6 offset:9216\n\t"
      "s_waitcnt lgkmcnt(9)\n\t"
      "v_mfma_f32_16x16x32_bf16 v[64:67], v[84:87], v[36:39], v[64:67]\n\t"
      "ds_read_b128 v[80:83], v6 offset:9728\n\t"
      "s_waitcnt lgkmcnt(8)\n\t"
      "v_mfma_f32_16x16x32_bf16 v[64:67], v[88:91], v[40:43], v[64:67]\n\t"
      "ds_read_b128 v[84:87], v6 offset:12288\n\t"
      "v_mfma_f32_16x16x32_bf16 v[64:67], v[92:95], v[44:47], v[64:67]\n\t"
      "ds_read_b128 v[88:91], v6 offset:12800\n\t"
      "s_waitcnt lgkmcnt(8)\n\t"
      "v_mfma_f32_16x16x32_bf16 v[68:71], v[96:99], v[32:35], 0\n\t"
      "ds_read_b128 v[92:95], v6 offset:13312\n\t"
      "v_mfma_f32_16x16x32_bf16 v[68:71], v[100:103], v[36:39], v[68:71]\n\t"
      "ds_read_b128 v[96:99], v6 offset:13824\n\t"
      "s_waitcnt lgkmcnt(8)\n\t"
      "v_mfma_f32_16x16x32_bf16 v[68:71], v[104:107], v[40:43], v[68:71]\n\t"
      "v_fmamk_f32 v48, v64, 0x3e0293ee, v8\n\t"
      "v_fmamk_f32 v49, v65, 0x3e0293ee, v8\n\t"
      "v_fmamk_f32 v50, v66, 0x3e0293ee, v8\n\t"
      "v_mfma_f32_16x16x32_bf16 v[68:71], v[108:111], v[44:47], v[68:71]\n\t"
      "v_fmamk_f32 v51, v67, 0x3e0293ee, v8\n\t"
      "v_exp_f32 v48, v48\n\t"
      "v_exp_f32 v49, v49\n\t"
      "s_waitcnt lgkmcnt(6)\n\t"
      "v_mfma_f32_16x16x32_bf16 v[72:75], v[112:115], v[32:35], 0\n\t"
      "v_exp_f32 v50, v50\n\t"
      "v_exp_f32 v51, v51\n\t"
      "v_mfma_f32_16x16x32_bf16 v[72:75], v[116:119], v[36:39], v[72:75]\n\t"
      "v_fmamk_f32 v52, v68, 0x3e0293ee, v8\n\t"
      "v_fmamk_f32 v53, v69, 0x3e0293ee, v8\n\t"
      "v_fmamk_f32 v54, v70, 0x3e0293ee, v8\n\t"
      "s_waitcnt lgkmcnt(4)\n\t"
      "v_mfma_f32_16x16x32_bf16 v[72:75], v[120:123], v[40:43], v[72:75]\n\t"
      "v_fmamk_f32 v55, v71, 0x3e0293ee, v8\n\t"
      "v_cvt_pk_bf16_f32 v24, v48, v49\n\t"
      "v_cvt_pk_bf16_f32 v25, v50, v51\n\t"
      "v_mfma_f32_16x16x32_bf16 v[72:75], v[80:83], v[44:47], v[72:75]\n\t"
      "v_exp_f32 v52, v52\n\t"
      "v_exp_f32 v53, v53\n\t"
      "v_exp_f32 v54, v54\n\t"
      "s_waitcnt lgkmcnt(2)\n\t"
      "v_mfma_f32_16x16x32_bf16 v[76:79], v[84:87], v[32:35], 0\n\t"
      "v_exp_f32 v55, v55\n\t"
      "v_mfma_f32_16x16x32_bf16 v[76:79], v[88:91], v[36:39], v[76:79]\n\t"
      "s_waitcnt lgkmcnt(0)\n\t"
      "v_mfma_f32_16x16x32_bf16 v[76:79], v[92:95], v[40:43], v[76:79]\n\t"
      "v_fmamk_f32 v56, v72, 0x3e0293ee, v8\n\t"
      "v_fmamk_f32 v57, v73, 0x3e0293ee, v8\n\t"
      "v_fmamk_f32 v58, v74, 0x3e0293ee, v8\n\t"
      "v_mfma_f32_16x16x32_bf16 v[76:79], v[96:99], v[44:47], v[76:79]\n\t"
      "v_fmamk_f32 v59, v75, 0x3e0293ee, v8\n\t"
      "v_cvt_pk_bf16_f32 v26, v52, v53\n\t"
      "v_cvt_pk_bf16_f32 v27, v54, v55\n\t"
      "v_add_u32 v6, s44, v2\n\t"
      "v_exp_f32 v56, v56\n\t"
      "v_exp_f32 v57, v57\n\t"
      "v_exp_f32 v58, v58\n\t"
      "v_exp_f32 v59, v59\n\t"
      "v_fmamk_f32 v60, v76, 0x3e0293ee, v8\n\t"
      "v_fmamk_f32 v61, v77, 0x3e0293ee, v8\n\t"
      "v_fmamk_f32 v62, v78, 0x3e0293ee, v8\n\t"
      "v_fmamk_f32 v63, v79, 0x3e0293ee, v8\n\t"
      "v_cvt_pk_bf16_f32 v28, v56, v57\n\t"
      "v_cvt_pk_bf16_f32 v29, v58, v59\n\t"
      "v_exp_f32 v60, v60\n\t"
      "v_exp_f32 v61, v61\n\t"
      "v_exp_f32 v62, v62\n\t"
      "v_exp_f32 v63, v63\n\t"
      "v_or3_b32 v10, v24, v25, v26\n\t"
      "v_cvt_pk_bf16_f32 v30, v60, v61\n\t"
      "v_cvt_pk_bf16_f32 v31, v62, v63\n\t"
      "v_or3_b32 v10, v10, v27, v28\n\t"
      "v_or3_b32 v10, v10, v29, v30\n\t"
      "v_bitop3_b32 v10, v10, v31, s49 bitop3:0xa8\n\t"
      "v_cmp_ne_u32 vcc, 0, v10\n\t"
      "s_cbranch_vccnz L_RARE%=\n\t"
      "L_BACK%=:\n\t"
      "s_set_gpr_idx_on s39, gpr_idx(DST)\n\t"
      "s_nop 0\n\t"
      "v_mov_b32 v128, v24\n\t"
      "v_mov_b32 v129, v25\n\t"
      "v_mov_b32 v130, v26\n\t"
      "v_mov_b32 v131, v27\n\t"
      "v_mov_b32 v132, v28\n\t"
      "v_mov_b32 v133, v29\n\t"
      "v_mov_b32 v134, v30\n\t"
      "v_mov_b32 v135, v31\n\t"
      "s_set_gpr_idx_off\n\t"
      "s_add_u32 s39, s39, 8\n\t"
      "s_cmp_eq_u32 s40, s41\n\t"
      "s_cbranch_scc0 L_QK%=\n\t"
      "L_PADevt%=:\n\t"
      "s_cbranch_execz L_CONS%=\n\t"
      "s_cmp_eq_u32 s40, 16\n\t"
      "s_cbranch_scc1 L_VST%=\n\t"
      "s_cmp_eq_u32 s42, 0\n\t"
      "s_cbranch_scc1 L_ISSUE%=\n\t"
      "s_cmp_eq_u32 s42, 3\n\t"
      "s_cbranch_scc1 L_FIRST%=\n\t"
      "s_waitcnt vmcnt(0)\n\t"
      "v_readfirstlane_b32 s48, v125\n\t"
      "v_readfirstlane_b32 s47, v14\n\t"
      "v_readfirstlane_b32 s46, v15\n\t"
      "s_add_u32 s45, s48, 1\n\t"
      "s_add_u32 s48, s48, 2\n\t"
      "s_cmp_eq_u32 s45, s47\n\t"
      "s_cselect_b32 s45, s48, s45\n\t"
      "s_mov_b64 exec, 1\n\t"
      "v_mov_b32 v14, s45\n\t"
      "v_mov_b32 v15, s46\n\t"
      "global_store_dwordx2 v9, v[14:15], s[30:31] sc1\n\t"
      "s_nop 1\n\t"
      "s_mov_b64 exec, -1\n\t"
      "s_mov_b32 s42, 0\n\t"
      "s_add_u32 s41, s40, 8\n\t"
      "s_branch L_QK%=\n\t"
      "L_ISSUE%=:\n\t"
      "global_load_dwordx2 v[124:125], v9, s[18:19] sc1\n\t"
      "s_mov_b32 s41, 16\n\t"
      "s_branch L_QK%=\n\t"
      "L_FIRST%=:\n\t"
      "global_load_dwordx2 v[124:125], v9, s[18:19] sc1\n\t"
      "global_load_dwordx2 v[14:15], v9, s[30:31] sc1\n\t"
      "s_mov_b32 s42, 2\n\t"
      "s_add_u32 s41, s40, 2\n\t"
      "s_branch L_QK%=\n\t"
      "L_PADrare%=:\n\t"
      "s_cbranch_execz L_CONS%=\n\t"
      "L_RARE%=:\n\t"
      "v_max3_f32 v10, v64, v65, v66\n\t"
      "v_max3_f32 v10, v10, v67, v68\n\t"
      "v_max3_f32 v10, v10, v69, v70\n\t"
      "v_max3_f32 v10, v10, v71, v72\n\t"
      "v_max3_f32 v10, v10, v73, v74\n\t"
      "v_max3_f32 v10, v10, v75, v76\n\t"
      "v_max3_f32 v10, v10, v77, v78\n\t"
      "v_max_f32 v10, v10, v79\n\t"
      "v_mov_b32 v12, v10\n\t"
      "s_nop 1\n\t"
      "v_permlane16_swap_b32 v10, v12\n\t"
      "s_nop 1\n\t"
      "v_max_f32 v10, v10, v12\n\t"
      "v_mov_b32 v12, v10\n\t"
      "s_nop 1\n\t"
      "v_permlane32_swap_b32 v10, v12\n\t"
      "s_nop 1\n\t"
      "v_max_f32 v10, v10, v12\n\t"
      "v_mov_b32 v12, 0xc1700000\n\t"
      "v_fmamk_f32 v10, v10, 0xbe0293ee, v12\n\t"
      "v_floor_f32 v10, v10\n\t"
      "v_min_f32 v10, v10, v8\n\t"
      "v_sub_f32 v13, v8, v10\n\t"
      "v_exp_f32 v12, -v13\n\t"
      "v_mov_b32 v8, v10\n\t"
      "v_mul_f32 v20, v12, v20\n\t"
      "v_fmamk_f32 v48, v64, 0x3e0293ee, v8\n\t"
      "v_fmamk_f32 v49, v65, 0x3e0293ee, v8\n\t"
      "v_fmamk_f32 v50, v66, 0x3e0293ee, v8\n\t"
      "v_fmamk_f32 v51, v67, 0x3e0293ee, v8\n\t"
      "v_exp_f32 v48, v48\n\t"
      "v_exp_f32 v49, v49\n\t"
      "v_exp_f32 v50, v50\n\t"
      "v_exp_f32 v51, v51\n\t"
      "v_fmamk_f32 v52, v68, 0x3e0293ee, v8\n\t"
      "v_fmamk_f32 v53, v69, 0x3e0293ee, v8\n\t"
      "v_fmamk_f32 v54, v70, 0x3e0293ee, v8\n\t"
      "v_fmamk_f32 v55, v71, 0x3e0293ee, v8\n\t"
      "v_exp_f32 v52, v52\n\t"
      "v_exp_f32 v53, v53\n\t"
      "v_exp_f32 v54, v54\n\t"
      "v_exp_f32 v55, v55\n\t"
      "v_fmamk_f32 v56, v72, 0x3e0293ee, v8\n\t"
      "v_fmamk_f32 v57, v73, 0x3e0293ee, v8\n\t"
      "v_fmamk_f32 v58, v74, 0x3e0293ee, v8\n\t"
      "v_fmamk_f32 v59, v75, 0x3e0293ee, v8\n\t"
      "v_exp_f32 v56, v56\n\t"
      "v_exp_f32 v57, v57\n\t"
      "v_exp_f32 v58, v58\n\t"
      "v_exp_f32 v59, v59\n\t"
      "v_fmamk_f32 v60, v76, 0x3e0293ee, v8\n\t"
      "v_fmamk_f32 v61, v77, 0x3e0293ee, v8\n\t"
      "v_fmamk_f32 v62, v78, 0x3e0293ee, v8\n\t"
      "v_fmamk_f32 v63, v79, 0x3e0293ee, v8\n\t"
      "v_exp_f32 v60, v60\n\t"
      "v_exp_f32 v61, v61\n\t"
      "v_exp_f32 v62, v62\n\t"
      "v_exp_f32 v63, v63\n\t"
      "s_nop 0\n\t"
      "v_cvt_pk_bf16_f32 v24, v48, v49\n\t"
      "v_cvt_pk_bf16_f32 v25, v50, v51\n\t"
      "v_cvt_pk_bf16_f32 v26, v52, v53\n\t"
      "v_cvt_pk_bf16_f32 v27, v54, v55\n\t"
      "v_cvt_pk_bf16_f32 v28, v56, v57\n\t"
      "v_cvt_pk_bf16_f32 v29, v58, v59\n\t"
      "v_cvt_pk_bf16_f32 v30, v60, v61\n\t"
      "v_cvt_pk_bf16_f32 v31, v62, v63\n\t"
      "s_cmp_eq_u32 s39, 0\n\t"
      "s_cbranch_scc1 L_BACK%=\n\t"
      "v_cvt_u32_f32 v13, v13\n\t"
      "v_min_u32 v13, 0xff, v13\n\t"
      "v_mul_u32_u24 v13, 0x800080, v13\n\t"
      "s_getpc_b64 s[20:21]\n\t"
      "L_FPC%=:\n\t"
      "s_lshl_b32 s52, s39, 3\n\t"
      "s_sub_u32 s52, L_FIXE%=-L_FPC%=, s52\n\t"
      "s_add_u32 s20, s20, s52\n\t"
      "s_addc_u32 s21, s21, 0\n\t"
      "s_setpc_b64 s[20:21]\n\t"
      "L_PADv%=:\n\t"
      "s_cbranch_execz L_LWARM%=\n\t"
      "L_VST%=:\n\t"
      "v_mfma_f32_16x16x32_bf16 v[20:23], v[16:19], v[24:27], v[20:23]\n\t"
      "v_mfma_f32_16x16x32_bf16 v[20:23], v[16:19], v[28:31], v[20:23]\n\t"
      "s_mov_b32 s55, 5\n\t"
      "s_mov_b32 s41, -1\n\t"
      "L_VINIT%=:\n\t"
      "v_mov_b32 v32, 0\n\t"
      "v_mov_b32 v33, 0\n\t"
      "v_mov_b64 v[34:35], v[32:33]\n\t"
      "v_mov_b64 v[36:37], v[32:33]\n\t"
      "v_mov_b64 v[38:39], v[32:33]\n\t"
      "v_mov_b64 v[40:41], v[32:33]\n\t"
      "v_mov_b64 v[42:43], v[32:33]\n\t"
      "v_mov_b64 v[44:45], v[32:33]\n\t"
      "v_mov_b64 v[46:47], v[32:33]\n\t"
      "s_mov_b32 s39, 0\n\t"
      "v_mov_b32 v6, v3\n\t"
      "L_V%=:\n\t"
      "s_set_gpr_idx_on s39, gpr_idx(SRC0)\n\t"
      "s_nop 0\n\t"
      "v_mov_b32 v24, v128\n\t"
      "v_mov_b32 v25, v129\n\t"
      "v_mov_b32 v26, v130\n\t"
      "v_mov_b32 v27, v131\n\t"
      "v_mov_b32 v28, v132\n\t"
      "v_mov_b32 v29, v133\n\t"
      "v_mov_b32 v30, v134\n\t"
      "v_mov_b32 v31, v135\n\t"
      "s_set_gpr_idx_off\n\t"
      "s_add_u32 s39, s39, 8\n\t"
      "v_xor_b32 v7, 32, v6\n\t"
      "s_barrier\n\t"
      "ds_read_b64_tr_b16 v[80:81], v6 offset:0\n\t"
      "ds_read_b64_tr_b16 v[82:83], v6 offset:2048\n\t"
      "ds_read_b64_tr_b16 v[84:85], v7 offset:0\n\t"
      "ds_read_b64_tr_b16 v[86:87], v7 offset:2048\n\t"
      "ds_read_b64_tr_b16 v[88:89], v6 offset:512\n\t"
      "ds_read_b64_tr_b16 v[90:91], v6 offset:2560\n\t"
      "ds_read_b64_tr_b16 v[92:93], v7 offset:512\n\t"
      "ds_read_b64_tr_b16 v[94:95], v7 offset:2560\n\t"
      "ds_read_b64_tr_b16 v[96:97], v6 offset:4096\n\t"
      "ds_read_b64_tr_b16 v[98:99], v6 offset:6144\n\t"
      "ds_read_b64_tr_b16 v[100:101], v7 offset:4096\n\t"
      "ds_read_b64_tr_b16 v[102:103], v7 offset:6144\n\t"
      "s_cmp_eq_u32 s39, 128\n\t"
      "s_waitcnt lgkmcnt(10)\n\t"
      "v_mfma_f32_16x16x32_bf16 v[32:35], v[80:83], v[24:27], v[32:35]\n\t"
      "ds_read_b64_tr_b16 v[104:105], v6 offset:4608\n\t"
      "ds_read_b64_tr_b16 v[106:107], v6 offset:6656\n\t"
      "s_waitcnt lgkmcnt(8)\n\t"
      "v_mfma_f32_16x16x32_bf16 v[36:39], v[84:87], v[24:27], v[36:39]\n\t"
      "ds_read_b64_tr_b16 v[108:109], v7 offset:4608\n\t"
      "ds_read_b64_tr_b16 v[110:111], v7 offset:6656\n\t"
      "v_mfma_f32_16x16x32_bf16 v[40:43], v[88:91], v[24:27], v[40:43]\n\t"
      "s_waitcnt lgkmcnt(6)\n\t"
      "v_mfma_f32_16x16x32_bf16 v[44:47], v[92:95], v[24:27], v[44:47]\n\t"
      "v_mfma_f32_16x16x32_bf16 v[32:35], v[96:99], v[28:31], v[32:35]\n\t"
      "s_waitcnt lgkmcnt(2)\n\t"
      "v_mfma_f32_16x16x32_bf16 v[36:39], v[100:103], v[28:31], v[36:39]\n\t"
      "v_mfma_f32_16x16x32_bf16 v[40:43], v[104:107], v[28:31], v[40:43]\n\t"
      "s_waitcnt lgkmcnt(0)\n\t"
      "v_mfma_f32_16x16x32_bf16 v[44:47], v[108:111], v[28:31], v[44:47]\n\t"
      "v_add_u32 v6, 0x2000, v6\n\t"
      "s_cbranch_scc1 L_VEND%=\n\t"
      "s_cmp_lg_u32 s39, s41\n\t"
      "s_cbranch_scc1 L_V%=\n\t"
      "L_PADst%=:\n\t"
      "s_cbranch_execz L_LWARM%=\n\t"
      "s_cmp_eq_u32 s39, 56\n\t"
      "s_cbranch_scc1 L_POLL%=\n\t"
      "s_cmp_eq_u32 s39, 88\n\t"
      "s_cbranch_scc1 L_FETCH%=\n\t"
      "s_waitcnt vmcnt(0)\n\t"
      "s_cmp_eq_u32 s39, 32\n\t"
      "s_cbranch_scc1 L_DRAINED%=\n\t"
      "s_cmp_gt_u32 s39, 88\n\t"
      "s_cbranch_scc1 L_PRE%=\n\t"
      "L_LOOK%=:\n\t"
      "v_readfirstlane_b32 s47, v124\n\t"
      "v_readfirstlane_b32 s48, v125\n\t"
      "s_cmp_eq_u32 s39, 40\n\t"
      "s_cbranch_scc1 L_FLAG%=\n\t"
      "s_cmp_eq_u32 s47, s46\n\t"
      "s_cbranch_scc1 L_CHK%=\n\t"
      "L_FLAG%=:\n\t"
      "s_mov_b32 s46, s47\n\t"
      "s_mov_b64 exec, 1\n\t"
      "v_mov_b32 v14, s45\n\t"
      "v_mov_b32 v15, s46\n\t"
      "global_store_dwordx2 v9, v[14:15], s[30:31] sc1\n\t"
      "s_nop 1\n\t"
      "s_mov_b64 exec, -1\n\t"
      "s_mov_b32 s41, 56\n\t"
      "s_cmp_eq_u32 s39, 40\n\t"
      "s_cbranch_scc1 L_V%=\n\t"
      "L_CHK%=:\n\t"
      "s_mov_b32 s41, 88\n\t"
      "s_cmp_eq_u32 s48, s45\n\t"
      "s_cbranch_scc1 L_V%=\n\t"
      "s_sub_u32 s51, s51, 1\n\t"
      "s_cbranch_scc1 L_V%=\n\t"
      "global_load_dwordx2 v[124:125], v9, s[18:19] sc1\n\t"
      "s_waitcnt vmcnt(0)\n\t"
      "s_branch L_LOOK%=\n\t"
      "L_DRAINED%=:\n\t"
      "s_mov_b32 s41, 88\n\t"
      "s_cmp_eq_u32 s38, 0\n\t"
      "s_cselect_b32 s41, 40, s41\n\t"
      "s_branch L_V%=\n\t"
      "L_POLL%=:\n\t"
      "global_load_dwordx2 v[124:125], v9, s[18:19] sc1\n\t"
      "s_mov_b32 s41, 80\n\t"
      "s_branch L_V%=\n\t"
      "L_FETCH%=:\n\t"
      "global_load_dwordx2 v[116:117], v11, s[34:35] sc1\n\t"
      "global_load_dwordx4 v[48:51], v5, s[34:35] offset:0 sc1\n\t"
      "global_load_dwordx4 v[52:55], v5, s[34:35] offset:1024 sc1\n\t"
      "s_mov_b32 s41, 120\n\t"
      "s_branch L_V%=\n\t"
      "L_PADpre%=:\n\t"
      "s_cbranch_execz L_LWARM%=\n\t"
      "L_PRE%=:\n\t"
      "v_min_f32 v10, v116, v8\n\t"
      "v_sub_f32 v114, v10, v116\n\t"
      "v_sub_f32 v112, v10, v8\n\t"
      "v_exp_f32 v114, v114\n\t"
      "v_exp_f32 v112, v112\n\t"
      "s_nop 0\n\t"
      "v_mul_f32 v10, v112, v20\n\t"
      "v_fmac_f32 v10, v114, v117\n\t"
      "v_rcp_f32 v10, v10\n\t"
      "s_nop 0\n\t"
      "v_mul_f32 v114, v114, v10\n\t"
      "v_mul_f32 v112, v112, v10\n\t"
      "v_lshlrev_b32 v64, 16, v48\n\t"
      "v_and_b32 v65, 0xffff0000, v48\n\t"
      "v_pk_mul_f32 v[64:65], v[64:65], v[114:115] op_sel_hi:[1,0]\n\t"
      "v_lshlrev_b32 v66, 16, v49\n\t"
      "v_and_b32 v67, 0xffff0000, v49\n\t"
      "v_pk_mul_f32 v[66:67], v[66:67], v[114:115] op_sel_hi:[1,0]\n\t"
      "v_lshlrev_b32 v68, 16, v50\n\t"
      "v_and_b32 v69, 0xffff0000, v50\n\t"
      "v_pk_mul_f32 v[68:69], v[68:69], v[114:115] op_sel_hi:[1,0]\n\t"
      "v_lshlrev_b32 v70, 16, v51\n\t"
      "v_and_b32 v71, 0xffff0000, v51\n\t"
      "v_pk_mul_f32 v[70:71], v[70:71], v[114:115] op_sel_hi:[1,0]\n\t"
      "v_lshlrev_b32 v72, 16, v52\n\t"
      "v_and_b32 v73, 0xffff0000, v52\n\t"
      "v_pk_mul_f32 v[72:73], v[72:73], v[114:115] op_sel_hi:[1,0]\n\t"
      "v_lshlrev_b32 v74, 16, v53\n\t"
      "v_and_b32 v75, 0xffff0000, v53\n\t"
      "v_pk_mul_f32 v[74:75], v[74:75], v[114:115] op_sel_hi:[1,0]\n\t"
      "v_lshlrev_b32 v76, 16, v54\n\t"
      "v_and_b32 v77, 0xffff0000, v54\n\t"
      "v_pk_mul_f32 v[76:77], v[76:77], v[114:115] op_sel_hi:[1,0]\n\t"
      "v_lshlrev_b32 v78, 16, v55\n\t"
      "v_and_b32 v79, 0xffff0000, v55\n\t"
      "v_pk_mul_f32 v[78:79], v[78:79], v[114:115] op_sel_hi:[1,0]\n\t"
      "s_branch L_V%=\n\t"
      "L_SEND%=:\n\t"
      "v_mov_b32 v120, v8\n\t"
      "v_mov_b32 v121, v20\n\t"
      "s_mov_b32 s55, 0\n\t"
      "s_mov_b32 s51, 0x100000\n\t"
      "s_mov_b32 s41, 32\n\t"
      "s_nop 3\n\t"
      "v_cvt_pk_bf16_f32 v56, v32, v33\n\t"
      "v_cvt_pk_bf16_f32 v57, v34, v35\n\t"
      "v_cvt_pk_bf16_f32 v58, v36, v37\n\t"
      "v_cvt_pk_bf16_f32 v59, v38, v39\n\t"
      "v_cvt_pk_bf16_f32 v60, v40, v41\n\t"
      "v_cvt_pk_bf16_f32 v61, v42, v43\n\t"
      "v_cvt_pk_bf16_f32 v62, v44, v45\n\t"
      "v_cvt_pk_bf16_f32 v63, v46, v47\n\t"
      "global_store_dwordx4 v5, v[56:59], s[16:17] sc1\n\t"
      "global_store_dwordx4 v5, v[60:63], s[16:17] offset:1024 sc1\n\t"
      "s_mov_b64 exec, 0xffff\n\t"
      "global_store_dwordx4 v11, v[120:123], s[16:17] sc1\n\t"
      "s_mov_b64 exec, -1\n\t"
      "s_branch L_VINIT%=\n\t"
      "L_PADfin%=:\n\t"
      "s_cbranch_execz L_LWARM%=\n\t"
      "L_VEND%=:\n\t"
      "s_cmp_eq_u32 s55, 5\n\t"
      "s_cbranch_scc1 L_SEND%=\n\t"
      "s_nop 1\n\t"
      "L_OUT%=:\n\t"
      "s_mov_b32 vcc_lo, 0xff00ff00\n\t"
      "s_mov_b32 vcc_hi, 0xff00ff00\n\t"
      "s_nop 0\n\t"
      "v_pk_fma_f32 v[32:33], v[32:33], v[112:113], v[64:65] op_sel_hi:[1,0,1]\n\t"
      "v_pk_fma_f32 v[34:35], v[34:35], v[112:113], v[66:67] op_sel_hi:[1,0,1]\n\t"
      "v_pk_fma_f32 v[36:37], v[36:37], v[112:113], v[68:69] op_sel_hi:[1,0,1]\n\t"
      "v_pk_fma_f32 v[38:39], v[38:39], v[112:113], v[70:71] op_sel_hi:[1,0,1]\n\t"
      "v_cvt_pk_bf16_f32 v48, v32, v33\n\t"
      "v_cvt_pk_bf16_f32 v49, v34, v35\n\t"
      "v_cvt_pk_bf16_f32 v50, v36, v37\n\t"
      "v_cvt_pk_bf16_f32 v51, v38, v39\n\t"
      "s_nop 1\n\t"
      "v_permlane16_swap_b32 v48, v50\n\t"
      "v_permlane16_swap_b32 v49, v51\n\t"
      "v_pk_fma_f32 v[40:41], v[40:41], v[112:113], v[72:73] op_sel_hi:[1,0,1]\n\t"
      "v_pk_fma_f32 v[42:43], v[42:43], v[112:113], v[74:75] op_sel_hi:[1,0,1]\n\t"
      "v_pk_fma_f32 v[44:45], v[44:45], v[112:113], v[76:77] op_sel_hi:[1,0,1]\n\t"
      "v_pk_fma_f32 v[46:47], v[46:47], v[112:113], v[78:79] op_sel_hi:[1,0,1]\n\t"
      "v_cvt_pk_bf16_f32 v52, v40, v41\n\t"
      "v_cvt_pk_bf16_f32 v53, v42, v43\n\t"
      "v_cvt_pk_bf16_f32 v54, v44, v45\n\t"
      "v_cvt_pk_bf16_f32 v55, v46, v47\n\t"
      "s_nop 1\n\t"
      "v_permlane16_swap_b32 v52, v54\n\t"
      "v_permlane16_swap_b32 v53, v55\n\t"
      "s_nop 1\n\t"
      "v_cndmask_b32_dpp v60, v48, v52, vcc row_ror:8 row_mask:0xf bank_mask:0xf\n\t"
      "v_cndmask_b32_dpp v61, v49, v53, vcc row_ror:8 row_mask:0xf bank_mask:0xf\n\t"
      "v_cndmask_b32_dpp v62, v50, v54, vcc row_ror:8 row_mask:0xf bank_mask:0xf\n\t"
      "v_cndmask_b32_dpp v63, v51, v55, vcc row_ror:8 row_mask:0xf bank_mask:0xf\n\t"
      "s_not_b64 vcc, vcc\n\t"
      "global_store_dwordx4 v4, v[60:63], s[26:27] offset:2048 sc1\n\t"
      "v_cndmask_b32_dpp v56, v52, v48, vcc row_ror:8 row_mask:0xf bank_mask:0xf\n\t"
      "v_cndmask_b32_dpp v57, v53, v49, vcc row_ror:8 row_mask:0xf bank_mask:0xf\n\t"
      "v_cndmask_b32_dpp v58, v54, v50, vcc row_ror:8 row_mask:0xf bank_mask:0xf\n\t"
      "v_cndmask_b32_dpp v59, v55, v51, vcc row_ror:8 row_mask:0xf bank_mask:0xf\n\t"
      "global_store_dwordx4 v4, v[56:59], s[26:27] sc1\n\t"
      "s_endpgm\n\t"
      "L_FIX%=:\n\t"
      "v_pk_sub_u16 v247, v247, v13 clamp\n\t"
      "v_pk_sub_u16 v246, v246, v13 clamp\n\t"
      "v_pk_sub_u16 v245, v245, v13 clamp\n\t"
      "v_pk_sub_u16 v244, v244, v13 clamp\n\t"
      "v_pk_sub_u16 v243, v243, v13 clamp\n\t"
      "v_pk_sub_u16 v242, v242, v13 clamp\n\t"
      "v_pk_sub_u16 v241, v241, v13 clamp\n\t"
      "v_pk_sub_u16 v240, v240, v13 clamp\n\t"
      "v_pk_sub_u16 v239, v239, v13 clamp\n\t"
      "v_pk_sub_u16 v238, v238, v13 clamp\n\t"
      "v_pk_sub_u16 v237, v237, v13 clamp\n\t"
      "v_pk_sub_u16 v236, v236, v13 clamp\n\t"
      "v_pk_sub_u16 v235, v235, v13 clamp\n\t"
      "v_pk_sub_u16 v234, v234, v13 clamp\n\t"
      "v_pk_sub_u16 v233, v233, v13 clamp\n\t"
      "v_pk_sub_u16 v232, v232, v13 clamp\n\t"
      "v_pk_sub_u16 v231, v231, v13 clamp\n\t"
      "v_pk_sub_u16 v230, v230, v13 clamp\n\t"
      "v_pk_sub_u16 v229, v229, v13 clamp\n\t"
      "v_pk_sub_u16 v228, v228, v13 clamp\n\t"
      "v_pk_sub_u16 v227, v227, v13 clamp\n\t"
      "v_pk_sub_u16 v226, v226, v13 clamp\n\t"
      "v_pk_sub_u16 v225, v225, v13 clamp\n\t"
      "v_pk_sub_u16 v224, v224, v13 clamp\n\t"
      "v_pk_sub_u16 v223, v223, v13 clamp\n\t"
      "v_pk_sub_u16 v222, v222, v13 clamp\n\t"
      "v_pk_sub_u16 v221, v221, v13 clamp\n\t"
      "v_pk_sub_u16 v220, v220, v13 clamp\n\t"
      "v_pk_sub_u16 v219, v219, v13 clamp\n\t"
      "v_pk_sub_u16 v218, v218, v13 clamp\n\t"
      "v_pk_sub_u16 v217, v217, v13 clamp\n\t"
      "v_pk_sub_u16 v216, v216, v13 clamp\n\t"
      "v_pk_sub_u16 v215, v215, v13 clamp\n\t"
      "v_pk_sub_u16 v214, v214, v13 clamp\n\t"
      "v_pk_sub_u16 v213, v213, v13 clamp\n\t"
      "v_pk_sub_u16 v212, v212, v13 clamp\n\t"
      "v_pk_sub_u16 v211, v211, v13 clamp\n\t"
      "v_pk_sub_u16 v210, v210, v13 clamp\n\t"
      "v_pk_sub_u16 v209, v209, v13 clamp\n\t"
      "v_pk_sub_u16 v208, v208, v13 clamp\n\t"
      "v_pk_sub_u16 v207, v207, v13 clamp\n\t"
      "v_pk_sub_u16 v206, v206, v13 clamp\n\t"
      "v_pk_sub_u16 v205, v205, v13 clamp\n\t"
      "v_pk_sub_u16 v204, v204, v13 clamp\n\t"
      "v_pk_sub_u16 v203, v203, v13 clamp\n\t"
      "v_pk_sub_u16 v202, v202, v13 clamp\n\t"
      "v_pk_sub_u16 v201, v201, v13 clamp\n\t"
      "v_pk_sub_u16 v200, v200, v13 clamp\n\t"
      "v_pk_sub_u16 v199, v199, v13 clamp\n\t"
      "v_pk_sub_u16 v198, v198, v13 clamp\n\t"
      "v_pk_sub_u16 v197, v197, v13 clamp\n\t"
      "v_pk_sub_u16 v196, v196, v13 clamp\n\t"
      "v_pk_sub_u16 v195, v195, v13 clamp\n\t"
      "v_pk_sub_u16 v194, v194, v13 clamp\n\t"
      "v_pk_sub_u16 v193, v193, v13 clamp\n\t"
      "v_pk_sub_u16 v192, v192, v13 clamp\n\t"
      "v_pk_sub_u16 v191, v191, v13 clamp\n\t"
      "v_pk_sub_u16 v190, v190, v13 clamp\n\t"
      "v_pk_sub_u16 v189, v189, v13 clamp\n\t"
      "v_pk_sub_u16 v188, v188, v13 clamp\n\t"
      "v_pk_sub_u16 v187, v187, v13 clamp\n\t"
      "v_pk_sub_u16 v186, v186, v13 clamp\n\t"
      "v_pk_sub_u16 v185, v185, v13 clamp\n\t"
      "v_pk_sub_u16 v184, v184, v13 clamp\n\t"
      "v_pk_sub_u16 v183, v183, v13 clamp\n\t"
      "v_pk_sub_u16 v182, v182, v13 clamp\n\t"
      "v_pk_sub_u16 v181, v181, v13 clamp\n\t"
      "v_pk_sub_u16 v180, v180, v13 clamp\n\t"
      "v_pk_sub_u16 v179, v179, v13 clamp\n\t"
      "v_pk_sub_u16 v178, v178, v13 clamp\n\t"
      "v_pk_sub_u16 v177, v177, v13 clamp\n\t"
      "v_pk_sub_u16 v176, v176, v13 clamp\n\t"
      "v_pk_sub_u16 v175, v175, v13 clamp\n\t"
      "v_pk_sub_u16 v174, v174, v13 clamp\n\t"
      "v_pk_sub_u16 v173, v173, v13 clamp\n\t"
      "v_pk_sub_u16 v172, v172, v13 clamp\n\t"
      "v_pk_sub_u16 v171, v171, v13 clamp\n\t"
      "v_pk_sub_u16 v170, v170, v13 clamp\n\t"
      "v_pk_sub_u16 v169, v169, v13 clamp\n\t"
      "v_pk_sub_u16 v168, v168, v13 clamp\n\t"
      "v_pk_sub_u16 v167, v167, v13 clamp\n\t"
      "v_pk_sub_u16 v166, v166, v13 clamp\n\t"
      "v_pk_sub_u16 v165, v165, v13 clamp\n\t"
      "v_pk_sub_u16 v164, v164, v13 clamp\n\t"
      "v_pk_sub_u16 v163, v163, v13 clamp\n\t"
      "v_pk_sub_u16 v162, v162, v13 clamp\n\t"
      "v_pk_sub_u16 v161, v161, v13 clamp\n\t"
      "v_pk_sub_u16 v160, v160, v13 clamp\n\t"
      "v_pk_sub_u16 v159, v159, v13 clamp\n\t"
      "v_pk_sub_u16 v158, v158, v13 clamp\n\t"
      "v_pk_sub_u16 v157, v157, v13 clamp\n\t"
      "v_pk_sub_u16 v156, v156, v13 clamp\n\t"
      "v_pk_sub_u16 v155, v155, v13 clamp\n\t"
      "v_pk_sub_u16 v154, v154, v13 clamp\n\t"
      "v_pk_sub_u16 v153, v153, v13 clamp\n\t"
      "v_pk_sub_u16 v152, v152, v13 clamp\n\t"
      "v_pk_sub_u16 v151, v151, v13 clamp\n\t"
      "v_pk_sub_u16 v150, v150, v13 clamp\n\t"
      "v_pk_sub_u16 v149, v149, v13 clamp\n\t"
      "v_pk_sub_u16 v148, v148, v13 clamp\n\t"
      "v_pk_sub_u16 v147, v147, v13 clamp\n\t"
      "v_pk_sub_u16 v146, v146, v13 clamp\n\t"
      "v_pk_sub_u16 v145, v145, v13 clamp\n\t"
      "v_pk_sub_u16 v144, v144, v13 clamp\n\t"
      "v_pk_sub_u16 v143, v143, v13 clamp\n\t"
      "v_pk_sub_u16 v142, v142, v13 clamp\n\t"
      "v_pk_sub_u16 v141, v141, v13 clamp\n\t"
      "v_pk_sub_u16 v140, v140, v13 clamp\n\t"
      "v_pk_sub_u16 v139, v139, v13 clamp\n\t"
      "v_pk_sub_u16 v138, v138, v13 clamp\n\t"
      "v_pk_sub_u16 v137, v137, v13 clamp\n\t"
      "v_pk_sub_u16 v136, v136, v13 clamp\n\t"
      "v_pk_sub_u16 v135, v135, v13 clamp\n\t"
      "v_pk_sub_u16 v134, v134, v13 clamp\n\t"
      "v_pk_sub_u16 v133, v133, v13 clamp\n\t"
      "v_pk_sub_u16 v132, v132, v13 clamp\n\t"
      "v_pk_sub_u16 v131, v131, v13 clamp\n\t"
      "v_pk_sub_u16 v130, v130, v13 clamp\n\t"
      "v_pk_sub_u16 v129, v129, v13 clamp\n\t"
      "v_pk_sub_u16 v128, v128, v13 clamp\n\t"
      "L_FIXE%=:\n\t"
      "s_branch L_BACK%=\n\t"
      :: "s"(__builtin_amdgcn_kernarg_segment_ptr()), "s"(blockIdx.x), "v"(threadIdx.x), "s"(ldsb)
      : "v1", "v2", "v3", "v4", "v5", "v6", "v7", "v8", "v9", "v10", "v11", "v12", "v13", "v14", "v15", "v16", "v17", "v18", "v19", "v20",
        "v21", "v22", "v23", "v24", "v25", "v26", "v27", "v28", "v29", "v30", "v31", "v32", "v33", "v34", "v35", "v36", "v37", "v38", "v39", "v40",
        "v41", "v42", "v43", "v44", "v45", "v46", "v47", "v48", "v49", "v50", "v51", "v52", "v53", "v54", "v55", "v56", "v57", "v58", "v59", "v60",
        "v61", "v62", "v63", "v64", "v65", "v66", "v67", "v68", "v69", "v70", "v71", "v72", "v73", "v74", "v75", "v76", "v77", "v78", "v79", "v80",
        "v81", "v82", "v83", "v84", "v85", "v86", "v87", "v88", "v89", "v90", "v91", "v92", "v93", "v94", "v95", "v96", "v97", "v98", "v99", "v100",
        "v101", "v102", "v103", "v104", "v105", "v106", "v107", "v108", "v109", "v110", "v111", "v112", "v113", "v114", "v115", "v116", "v117", "v118", "v119", "v120",
        "v121", "v122", "v123", "v124", "v125", "v126", "v127", "v128", "v129", "v130", "v131", "v132", "v133", "v134", "v135", "v136", "v137", "v138", "v139", "v140",
        "v141", "v142", "v143", "v144", "v145", "v146", "v147", "v148", "v149", "v150", "v151", "v152", "v153", "v154", "v155", "v156", "v157", "v158", "v159", "v160",
        "v161", "v162", "v163", "v164", "v165", "v166", "v167", "v168", "v169", "v170", "v171", "v172", "v173", "v174", "v175", "v176", "v177", "v178", "v179", "v180",
        "v181", "v182", "v183", "v184", "v185", "v186", "v187", "v188", "v189", "v190", "v191", "v192", "v193", "v194", "v195", "v196", "v197", "v198", "v199", "v200",
        "v201", "v202", "v203", "v204", "v205", "v206", "v207", "v208", "v209", "v210", "v211", "v212", "v213", "v214", "v215", "v216", "v217", "v218", "v219", "v220",
        "v221", "v222", "v223", "v224", "v225", "v226", "v227", "v228", "v229", "v230", "v231", "v232", "v233", "v234", "v235", "v236", "v237", "v238", "v239", "v240",
        "v241", "v242", "v243", "v244", "v245", "v246", "v247", "v248", "v249", "v250", "v251", "v252", "v253", "v254", "v255", "s16", "s17", "s18", "s19", "s20",
        "s21", "s22", "s23", "s24", "s25", "s26", "s27", "s28", "s29", "s30", "s31", "s34", "s35", "s36", "s37", "s38", "s39", "s40", "s41", "s42",
        "s43", "s44", "s45", "s46", "s47", "s48", "s49", "s50", "s51", "s52", "s53", "s54", "s55", "vcc", "scc", "memory");
}

extern "C" void kernel_launch(void* const* d_in, const int* in_sizes, int n_in, void* d_out, int out_size, void* d_ws, size_t ws_size,
                              hipStream_t stream) {
  gqa_kernel<<<256, 512, 0, stream>>>((const __hip_bfloat16*)d_in[0], (const __hip_bfloat16*)d_in[1], (const __hip_bfloat16*)d_in[2],
                                      (__hip_bfloat16*)d_out, (char*)d_ws);
}
```
